```python
import torch

def _softmax(x, axis=-1):
    return torch.softmax(x, dim=axis)
def _gelu(x):
    return torch.nn.functional.gelu(x, approximate="tanh")
def _silu(x):
    return torch.nn.functional.silu(x)
def _sigmoid(x):
    return torch.sigmoid(x)
def _layernorm(x, axis=-1, eps=1e-5):
    m = x.mean(dim=axis, keepdim=True)
    v = x.var(dim=axis, keepdim=True, unbiased=False)
    return (x - m) / torch.sqrt(v + eps)
def _scatter_add(tgt, idx, src, axis=0):
    return tgt.clone().index_add_(axis, idx, src)
def _segment_sum(src, seg, n):
    out = torch.zeros((n,) + src.shape[1:], dtype=src.dtype)
    return out.index_add_(0, seg, src)
def _pad(x, pw):
    flat = tuple(v for pair in reversed(pw) for v in pair)
    return torch.nn.functional.pad(x, flat)

kernel_name = 'standalone__instancenorm__prompt'

def setup_inputs():

    _w = (lambda a: (lambda t: t.to(torch.bfloat16) if t.is_floating_point() else t)(torch.as_tensor(a)))
    B, C, H, W = 2, 448, 2048, 16
    return {
        "x": _w(abs((lambda *s: torch.randn(*s, dtype=torch.bfloat16) * 0.05)(B, C, H, W))),
        "g": _w((lambda *s: torch.randn(*s, dtype=torch.bfloat16) * 0.05)(C) + 1.0),
        "b": _w((lambda *s: torch.randn(*s, dtype=torch.bfloat16) * 0.4)(C)),
    }

def reference(x, g, b):
    mu = x.mean(axis=(-2, -1), keepdims=True)
    var = ((x - mu) ** 2).mean(axis=(-2, -1), keepdims=True)
    xhat = (x - mu) / torch.sqrt(var + 1e-5)
    return xhat * g.reshape(1, -1, 1, 1) + b.reshape(1, -1, 1, 1)
```

```cpp
#include <hip/hip_runtime.h>
#include <hip/hip_bf16.h>
#include <stdint.h>

#define TPB 256
#define INST_ELEMS 32768
#define INST_VECS (INST_ELEMS / 8)

__device__ __forceinline__ uint32_t cvt_pk_bf16(float lo, float hi) {
    uint32_t r;
    asm("v_cvt_pk_bf16_f32 %0, %1, %2" : "=v"(r) : "v"(lo), "v"(hi));
    return r;
}
__device__ __forceinline__ float bf_lo(uint32_t w) { return __uint_as_float(w << 16); }
__device__ __forceinline__ float bf_hi(uint32_t w) { return __uint_as_float(w & 0xffff0000u); }

__device__ __forceinline__ float wave_sum(float v) {
#pragma unroll
    for (int off = 32; off > 0; off >>= 1) v += __shfl_xor(v, off, 64);
    return v;
}

template <int NT>
__device__ __forceinline__ float block_sum(float v, float* red) {
    constexpr int NW = NT / 64;
    v = wave_sum(v);
    if ((threadIdx.x & 63) == 0) red[threadIdx.x >> 6] = v;
    __syncthreads();
    float s = 0.f;
#pragma unroll
    for (int i = 0; i < NW; i++) s += red[i];
    return s;
}

template <int NT>
__global__ __launch_bounds__(NT) void inorm_fast(const uint4* __restrict__ x, const uint16_t* __restrict__ g,
                                                 const uint16_t* __restrict__ b, uint4* __restrict__ y, int C) {
    constexpr int NV = INST_VECS / NT;
    const int inst = blockIdx.x;
    const uint4* xp = x + (size_t)inst * INST_VECS + threadIdx.x;
    uint4* yp = y + (size_t)inst * INST_VECS + threadIdx.x;
    __shared__ float red1[NT / 64];
    __shared__ float red2[NT / 64];
    uint4 v[NV];
#pragma unroll
    for (int i = 0; i < NV; i++) v[i] = xp[i * NT];
    float s0 = 0.f, s1 = 0.f;
#pragma unroll
    for (int i = 0; i < NV; i++) {
        s0 += bf_lo(v[i].x); s1 += bf_hi(v[i].x);
        s0 += bf_lo(v[i].y); s1 += bf_hi(v[i].y);
        s0 += bf_lo(v[i].z); s1 += bf_hi(v[i].z);
        s0 += bf_lo(v[i].w); s1 += bf_hi(v[i].w);
    }
    const int c = inst % C;
    const float gf = __uint_as_float((uint32_t)g[c] << 16);
    const float bf = __uint_as_float((uint32_t)b[c] << 16);
    float sum = block_sum<NT>(s0 + s1, red1);
    const float mu = sum * (1.0f / INST_ELEMS);
    float q0 = 0.f, q1 = 0.f;
#pragma unroll
    for (int i = 0; i < NV; i++) {
        const uint32_t* w = reinterpret_cast<const uint32_t*>(&v[i]);
#pragma unroll
        for (int j = 0; j < 4; j++) {
            float dl = bf_lo(w[j]) - mu, dh = bf_hi(w[j]) - mu;
            q0 = fmaf(dl, dl, q0); q1 = fmaf(dh, dh, q1);
        }
    }
    float sq = block_sum<NT>(q0 + q1, red2);
    const float var = sq * (1.0f / INST_ELEMS);
    const float inv = 1.0f / sqrtf(var + 1e-5f);
    const float k = inv * gf;
#pragma unroll
    for (int i = 0; i < NV; i++) {
        const uint32_t* w = reinterpret_cast<const uint32_t*>(&v[i]);
        uint4 o;
        uint32_t* ow = reinterpret_cast<uint32_t*>(&o);
#pragma unroll
        for (int j = 0; j < 4; j++) {
            ow[j] = cvt_pk_bf16(fmaf(bf_lo(w[j]) - mu, k, bf), fmaf(bf_hi(w[j]) - mu, k, bf));
        }
        yp[i * NT] = o;
    }
}

__global__ __launch_bounds__(256) void inorm_generic(const uint16_t* __restrict__ x, const uint16_t* __restrict__ g,
                                                     const uint16_t* __restrict__ b, uint16_t* __restrict__ y, int C, int n) {
    __shared__ float red1[4];
    __shared__ float red2[4];
    const int inst = blockIdx.x;
    const uint16_t* xp = x + (size_t)inst * n;
    uint16_t* yp = y + (size_t)inst * n;
    float s = 0.f;
    for (int i = threadIdx.x; i < n; i += 256) s += __uint_as_float((uint32_t)xp[i] << 16);
    float sum = block_sum<256>(s, red1);
    const float mu = sum / (float)n;
    float q = 0.f;
    for (int i = threadIdx.x; i < n; i += 256) {
        float d = __uint_as_float((uint32_t)xp[i] << 16) - mu;
        q = fmaf(d, d, q);
    }
    float sq = block_sum<256>(q, red2);
    const int c = inst % C;
    const float gf = __uint_as_float((uint32_t)g[c] << 16);
    const float bf = __uint_as_float((uint32_t)b[c] << 16);
    const float var = sq / (float)n;
    const float k = (1.0f / sqrtf(var + 1e-5f)) * gf;
    for (int i = threadIdx.x; i < n; i += 256) {
        float d = __uint_as_float((uint32_t)xp[i] << 16) - mu;
        yp[i] = (uint16_t)(cvt_pk_bf16(fmaf(d, k, bf), 0.f) & 0xffffu);
    }
}

extern "C" void kernel_launch(void* const* d_in, const int* in_sizes, int n_in,
                              void* d_out, int out_size, void* d_ws, size_t ws_size,
                              hipStream_t stream) {
    const int total = in_sizes[0];
    const int C = in_sizes[1];
    const int n_inst = 2 * C;
    const int per = total / n_inst;
    const uint16_t* g = (const uint16_t*)d_in[1];
    const uint16_t* b = (const uint16_t*)d_in[2];
    if (per == INST_ELEMS && (size_t)n_inst * per == (size_t)total) {
        inorm_fast<TPB><<<n_inst, TPB, 0, stream>>>((const uint4*)d_in[0], g, b, (uint4*)d_out, C);
    } else {
        inorm_generic<<<n_inst, 256, 0, stream>>>((const uint16_t*)d_in[0], g, b, (uint16_t*)d_out, C, per);
    }
}
```

```cpp
#include <hip/hip_runtime.h>
#include <hip/hip_bf16.h>
#include <stdint.h>

#define TPB 256
#define INST_ELEMS 32768
#define INST_VECS (INST_ELEMS / 8)

typedef unsigned int u32x4 __attribute__((ext_vector_type(4)));

__device__ __forceinline__ uint32_t cvt_pk_bf16(float lo, float hi) {
    uint32_t r;
    asm("v_cvt_pk_bf16_f32 %0, %1, %2" : "=v"(r) : "v"(lo), "v"(hi));
    return r;
}
__device__ __forceinline__ float bf_lo(uint32_t w) { return __uint_as_float(w << 16); }
__device__ __forceinline__ float bf_hi(uint32_t w) { return __uint_as_float(w & 0xffff0000u); }

__device__ __forceinline__ float wave_sum(float v) {
#pragma unroll
    for (int off = 32; off > 0; off >>= 1) v += __shfl_xor(v, off, 64);
    return v;
}

template <int NT>
__device__ __forceinline__ float block_sum(float v, float* red) {
    constexpr int NW = NT / 64;
    v = wave_sum(v);
    if ((threadIdx.x & 63) == 0) red[threadIdx.x >> 6] = v;
    __syncthreads();
    float s = 0.f;
#pragma unroll
    for (int i = 0; i < NW; i++) s += red[i];
    return s;
}

template <int NT>
__global__ __launch_bounds__(NT, 4) void inorm_fast(const u32x4* __restrict__ x, const uint16_t* __restrict__ g,
                                                    const uint16_t* __restrict__ b, u32x4* __restrict__ y, int C) {
    constexpr int NV = INST_VECS / NT;
    const int inst = blockIdx.x;
    const u32x4* xp = x + (size_t)inst * INST_VECS + threadIdx.x;
    u32x4* yp = y + (size_t)inst * INST_VECS + threadIdx.x;
    __shared__ float red1[NT / 64];
    __shared__ float red2[NT / 64];
    u32x4 v[NV];
    { const int slot = inst >> 8; if (slot == 0) __builtin_amdgcn_s_setprio(3); else if (slot == 1) __builtin_amdgcn_s_setprio(2); else if (slot == 2) __builtin_amdgcn_s_setprio(1); }
#pragma unroll
    for (int i = 0; i < NV; i++) v[i] = __builtin_nontemporal_load(xp + i * NT);
    __builtin_amdgcn_sched_barrier(0);
    __builtin_amdgcn_s_setprio(0);
    const int c = inst % C;
    const float gf = __uint_as_float((uint32_t)g[c] << 16);
    const float bf = __uint_as_float((uint32_t)b[c] << 16);
    float s0 = 0.f, s1 = 0.f;
#pragma unroll
    for (int i = 0; i < NV; i++) {
#pragma unroll
        for (int j = 0; j < 4; j++) { s0 += bf_lo(v[i][j]); s1 += bf_hi(v[i][j]); }
    }
    float sum = block_sum<NT>(s0 + s1, red1);
    const float mu = sum * (1.0f / INST_ELEMS);
#pragma unroll
    for (int i = 0; i < NV; i++) {
#pragma unroll
        for (int j = 0; j < 4; j++) asm volatile("" : "+v"(v[i][j]));
    }
    float q0 = 0.f, q1 = 0.f;
#pragma unroll
    for (int i = 0; i < NV; i++) {
#pragma unroll
        for (int j = 0; j < 4; j++) {
            float dl = bf_lo(v[i][j]) - mu, dh = bf_hi(v[i][j]) - mu;
            q0 = fmaf(dl, dl, q0); q1 = fmaf(dh, dh, q1);
        }
    }
    float sq = block_sum<NT>(q0 + q1, red2);
    const float var = sq * (1.0f / INST_ELEMS);
    const float inv = 1.0f / sqrtf(var + 1e-5f);
    const float k = inv * gf;
#pragma unroll
    for (int i = 0; i < NV; i++) {
#pragma unroll
        for (int j = 0; j < 4; j++) asm volatile("" : "+v"(v[i][j]));
    }
#pragma unroll
    for (int i = 0; i < NV; i++) {
        u32x4 o;
#pragma unroll
        for (int j = 0; j < 4; j++) {
            o[j] = cvt_pk_bf16(fmaf(bf_lo(v[i][j]) - mu, k, bf), fmaf(bf_hi(v[i][j]) - mu, k, bf));
        }
        __builtin_nontemporal_store(o, yp + i * NT);
    }
}

__global__ __launch_bounds__(256) void inorm_generic(const uint16_t* __restrict__ x, const uint16_t* __restrict__ g,
                                                     const uint16_t* __restrict__ b, uint16_t* __restrict__ y, int C, int n) {
    __shared__ float red1[4];
    __shared__ float red2[4];
    const int inst = blockIdx.x;
    const uint16_t* xp = x + (size_t)inst * n;
    uint16_t* yp = y + (size_t)inst * n;
    float s = 0.f;
    for (int i = threadIdx.x; i < n; i += 256) s += __uint_as_float((uint32_t)xp[i] << 16);
    float sum = block_sum<256>(s, red1);
    const float mu = sum / (float)n;
    float q = 0.f;
    for (int i = threadIdx.x; i < n; i += 256) {
        float d = __uint_as_float((uint32_t)xp[i] << 16) - mu;
        q = fmaf(d, d, q);
    }
    float sq = block_sum<256>(q, red2);
    const int c = inst % C;
    const float gf = __uint_as_float((uint32_t)g[c] << 16);
    const float bf = __uint_as_float((uint32_t)b[c] << 16);
    const float var = sq / (float)n;
    const float k = (1.0f / sqrtf(var + 1e-5f)) * gf;
    for (int i = threadIdx.x; i < n; i += 256) {
        float d = __uint_as_float((uint32_t)xp[i] << 16) - mu;
        yp[i] = (uint16_t)(cvt_pk_bf16(fmaf(d, k, bf), 0.f) & 0xffffu);
    }
}

extern "C" void kernel_launch(void* const* d_in, const int* in_sizes, int n_in,
                              void* d_out, int out_size, void* d_ws, size_t ws_size,
                              hipStream_t stream) {
    const int total = in_sizes[0];
    const int C = in_sizes[1];
    const int n_inst = 2 * C;
    const int per = total / n_inst;
    const uint16_t* g = (const uint16_t*)d_in[1];
    const uint16_t* b = (const uint16_t*)d_in[2];
    if (per == INST_ELEMS && (size_t)n_inst * per == (size_t)total) {
        inorm_fast<TPB><<<n_inst, TPB, 0, stream>>>((const u32x4*)d_in[0], g, b, (u32x4*)d_out, C);
    } else {
        inorm_generic<<<n_inst, 256, 0, stream>>>((const uint16_t*)d_in[0], g, b, (uint16_t*)d_out, C, per);
    }
}
```
